```python
import torch

def _softmax(x, axis=-1):
    return torch.softmax(x, dim=axis)
def _gelu(x):
    return torch.nn.functional.gelu(x, approximate="tanh")
def _silu(x):
    return torch.nn.functional.silu(x)
def _sigmoid(x):
    return torch.sigmoid(x)
def _layernorm(x, axis=-1, eps=1e-5):
    m = x.mean(dim=axis, keepdim=True)
    v = x.var(dim=axis, keepdim=True, unbiased=False)
    return (x - m) / torch.sqrt(v + eps)
def _scatter_add(tgt, idx, src, axis=0):
    return tgt.clone().index_add_(axis, idx, src)
def _segment_sum(src, seg, n):
    out = torch.zeros((n,) + src.shape[1:], dtype=src.dtype)
    return out.index_add_(0, seg, src)
def _pad(x, pw):
    flat = tuple(v for pair in reversed(pw) for v in pair)
    return torch.nn.functional.pad(x, flat)

kernel_name = 'gated_mix__spectral_fnet__mha_block__prompt'

def setup_inputs():

    _w = (lambda a: (lambda t: t.to(torch.bfloat16) if t.is_floating_point() else t)(torch.as_tensor(a)))
    def _A_setup():
        B, S, D = 1, 2048, 640
        ns = torch.arange(S)[:, None] * torch.arange(S)[None, :]
        nd = torch.arange(D)[:, None] * torch.arange(D)[None, :]
        Ws_re = torch.cos(-2.0 * torch.pi * ns / S).to(float)
        Ws_im = torch.sin(-2.0 * torch.pi * ns / S).to(float)
        Wd_re = torch.cos(-2.0 * torch.pi * nd / D).to(float)
        Wd_im = torch.sin(-2.0 * torch.pi * nd / D).to(float)
        return {
            "x": _w((lambda *s: torch.randn(*s, dtype=torch.bfloat16) * 0.05)(B, S, D)),
            "dft_seq_re": _w(Ws_re),
            "dft_seq_im": _w(Ws_im),
            "dft_hid_re": _w(Wd_re),
            "dft_hid_im": _w(Wd_im),
        }
    _A_in = _A_setup()

    def _B_setup():
        B, S, D, H = 1, 2048, 640, 32
        return {
            "x": _w((lambda *s: torch.randn(*s, dtype=torch.bfloat16) * 0.05)(B, S, D)),
            "wqkv": _w((lambda *s: torch.randn(*s, dtype=torch.bfloat16) * 1.6)(D, 3 * D)),
            "wo": _w((lambda *s: torch.randn(*s, dtype=torch.bfloat16) * 1.6)(D, D)),
        }
    _B_in = _B_setup()

    return {
        "A_x": _A_in["x"],
        "A_dft_seq_re": _A_in["dft_seq_re"],
        "A_dft_seq_im": _A_in["dft_seq_im"],
        "A_dft_hid_re": _A_in["dft_hid_re"],
        "A_dft_hid_im": _A_in["dft_hid_im"],
        "B_x": _B_in["x"],
        "B_wqkv": _B_in["wqkv"],
        "B_wo": _B_in["wo"],
        "gate_w": _w((lambda *s: torch.randn(*s, dtype=torch.bfloat16) * 0.05)(640) * 16.0),
    }

def reference(A_x, A_dft_seq_re, A_dft_seq_im, A_dft_hid_re, A_dft_hid_im, B_x, B_wqkv, B_wo, gate_w):
    def _A_ref(x, dft_seq_re, dft_seq_im, dft_hid_re, dft_hid_im):
        a = torch.einsum("bsd,ed->bse", x, dft_hid_re)
        b = torch.einsum("bsd,ed->bse", x, dft_hid_im)
        y = torch.einsum("ts,bse->bte", dft_seq_re, a) - torch.einsum("ts,bse->bte", dft_seq_im, b)
        return y

    def _B_ref(x, wqkv, wo):
        B, S, D = x.shape
        H = 32
        Dh = D // H
        qkv = (lambda x, *d: x.permute(*d))((x @ wqkv).reshape(B, S, 3, H, Dh), 2, 0, 3, 1, 4)
        q, k, v = qkv[0], qkv[1], qkv[2]
        scores = torch.einsum("bhqd,bhkd->bhqk", q, k) * (Dh ** -0.5)
        p = _softmax(scores, axis=-1)
        o = torch.einsum("bhqk,bhkd->bhqd", p, v)
        return (lambda x, *d: x.permute(*d))(o, 0, 2, 1, 3).reshape(B, S, D) @ wo

    x = A_x
    g = _sigmoid((x * gate_w).sum(-1, keepdims=True))
    a = _A_ref(x, A_dft_seq_re, A_dft_seq_im, A_dft_hid_re, A_dft_hid_im)
    b = _B_ref(x, B_wqkv, B_wo)
    return g * a + (1 - g) * b
```

```cpp
#include <hip/hip_runtime.h>
#include <hip/hip_bf16.h>

typedef unsigned short u16;
typedef __attribute__((ext_vector_type(8))) short bf16x8;
typedef __attribute__((ext_vector_type(4))) short s16x4;
typedef __attribute__((ext_vector_type(2))) short s16x2;
typedef __attribute__((ext_vector_type(16))) float f32x16;
typedef __attribute__((ext_vector_type(4))) float f32x4;
typedef __attribute__((ext_vector_type(2))) float f32x2;
typedef __attribute__((ext_vector_type(4))) unsigned u32x4;
typedef __attribute__((ext_vector_type(2))) unsigned u32x2;
typedef __attribute__((ext_vector_type(2))) __bf16 bf16x2_t;
typedef __amdgpu_buffer_rsrc_t rsrc_t;
#define LAS __attribute__((address_space(3)))
#define DI __device__ __forceinline__
#define MFMA32(a, b, c) __builtin_amdgcn_mfma_f32_32x32x16_bf16((a), (b), (c), 0, 0, 0)
#define MFMA16(a, b, c) __builtin_amdgcn_mfma_f32_16x16x32_bf16((a), (b), (c), 0, 0, 0)

constexpr int SEQ = 2048, DM = 640, NH = 32;
constexpr size_t WS_Q1 = 0;
constexpr size_t WS_Q2 = WS_Q1 + 32 * 64 * 1024;
constexpr int KVG = 11264;
constexpr size_t WS_KV = WS_Q2 + 32 * 64 * 512;
constexpr size_t WS_KN = WS_KV + (size_t)32 * 16 * KVG;
constexpr size_t WS_AB = WS_KN + 32 * 64 * 4;
constexpr size_t WS_G = WS_AB + (size_t)40 * 128 * 1024;
constexpr size_t WS_O = WS_G + 2048 * 4;
constexpr size_t WS_Y = WS_O + (size_t)64 * 40 * 1024;
constexpr size_t WS_END = WS_Y + (size_t)2 * 64 * 160 * 32 * 8;

DI unsigned rfl(unsigned x) { return __builtin_amdgcn_readfirstlane(x); }
DI unsigned pk(float lo, float hi) {
  f32x2 v = {lo, hi};
  bf16x2_t b = __builtin_convertvector(v, bf16x2_t);
  return __builtin_bit_cast(unsigned, b);
}
DI float dot2(unsigned a, unsigned b, float c) {
  return __builtin_amdgcn_fdot2_f32_bf16(__builtin_bit_cast(bf16x2_t, a), __builtin_bit_cast(bf16x2_t, b), c, false);
}
DI rsrc_t mkrs(const void* p, unsigned bytes) { return __builtin_amdgcn_make_buffer_rsrc((void*)p, (short)0, (int)bytes, 0x00020000); }
template <int OFF>
DI void dma16(unsigned m0v, unsigned voff, rsrc_t rs, unsigned soff) {
  asm volatile("s_mov_b32 m0, %0\n\ts_nop 0\n\tbuffer_load_dwordx4 %1, %2, %3 offen offset:%4 lds"
               :: "s"(m0v), "v"(voff), "s"(rs), "s"(soff), "i"(OFF) : "memory");
}
#define WAIT_BAR(N) asm volatile("s_waitcnt vmcnt(" #N ") lgkmcnt(0)\n\ts_barrier" ::: "memory")
DI bf16x8 ldsr128(const LAS char* p) { return *(const LAS bf16x8*)p; }
DI s16x4 ldstr(const LAS char* p) { return __builtin_amdgcn_ds_read_tr16_b64_v4i16((LAS s16x4*)p); }
DI bf16x8 cat(s16x4 a, s16x4 b) { return __builtin_shufflevector(a, b, 0, 1, 2, 3, 4, 5, 6, 7); }
DI bf16x8 asfrag(u32x4 v) { return __builtin_bit_cast(bf16x8, v); }

constexpr int K1_X = 0, K1_W = 32768, K1_H = K1_W + 9216, K1_STAGE = K1_H + 8192, K1_GW = 3 * K1_STAGE, K1_LDS = K1_GW + 2048;

__global__ __launch_bounds__(512, 2) void k1_proj(const u16* __restrict__ x, const u16* __restrict__ hr, const u16* __restrict__ hi,
                                                  const u16* __restrict__ wqkv, const u16* __restrict__ gw, char* __restrict__ ws) {
  __shared__ __attribute__((aligned(16))) char smem[K1_LDS];
  const LAS char* lds = (const LAS char*)smem;
  const unsigned ldsb = (unsigned)(size_t)lds;
  const int tid = threadIdx.x, lane = tid & 63, r = lane & 31, hh = lane >> 5;
  const int w = rfl(tid >> 6);
  const int bid = blockIdx.x, h = (bid & 7) * 4 + ((bid >> 3) & 3), j = bid >> 5;
  const int et0 = h, et1 = 32 + (h >> 2);
  const bool extra = (w >> 1) == (h & 3);
  const bool gate = (h == 0);

  const rsrc_t rsX = mkrs(x + (size_t)(j * 256 + w * 32) * DM, 32 * DM * 2);
  const int etw = (w >> 2) ? et1 : et0;
  const u16* hsrc = (etw < 20) ? hr + (size_t)etw * 32 * DM : hi + (size_t)(etw - 20) * 32 * DM;
  const rsrc_t rsH = mkrs(hsrc + (size_t)(w & 3) * 8 * DM, 8 * DM * 2);
  const rsrc_t rsW = mkrs((const char*)wqkv + ((40 * h) & ~15), DM * 3 * DM * 2 - ((40 * h) & ~15));
  const rsrc_t rsG = mkrs(gw, DM * 2);
  const unsigned vx0 = (lane >> 3) * 1280 + (((lane & 7) ^ (lane >> 4)) << 4);
  const unsigned vx1 = vx0 ^ 64;
  const unsigned vh = (w & 1) ? vx1 : vx0;
  unsigned vw, vw8;
  { int g = w * 64 + lane, k = g / 9, pc = g % 9; vw = k * 3840 + (pc / 3) * 1280 + (pc % 3) * 16;
    g = 512 + lane; k = g / 9; pc = g % 9; vw8 = k * 3840 + (pc / 3) * 1280 + (pc % 3) * 16; }

  auto issue = [&](int kb, int slot) {
    const unsigned sb = ldsb + slot * K1_STAGE;
    if (w == 0) dma16<0>(sb + K1_W + 8192, vw8, rsW, kb * 64 * 3840);
    dma16<0>(sb + K1_W + w * 1024, vw, rsW, kb * 64 * 3840);
    dma16<0>(sb + K1_H + w * 1024, vh, rsH, kb * 128);
    dma16<0>(sb + K1_X + w * 4096, vx0, rsX, kb * 128);
    dma16<0>(sb + K1_X + w * 4096 + 1024, vx1, rsX, kb * 128 + 8 * 1280);
    dma16<0>(sb + K1_X + w * 4096 + 2048, vx0, rsX, kb * 128 + 16 * 1280);
    dma16<0>(sb + K1_X + w * 4096 + 3072, vx1, rsX, kb * 128 + 24 * 1280);
  };
  if (gate && w == 1) { dma16<0>(ldsb + K1_GW, lane * 16, rsG, 0); dma16<1024>(ldsb + K1_GW, lane * 16, rsG, 0); }
  issue(0, 0); issue(1, 1); issue(2, 2);

  const int f = (r >> 1) & 7;
  const int ra = r * 128 + ((hh ^ f) << 4);
  int wo0, wo1;
  { const int mb = (lane >> 4) & 1, q = (lane & 15) >> 2, p = lane & 3, cg = 4 * mb + p, odd = (h & 1) * 8;
    const int c0 = cg < 5 ? 8 * cg : (cg == 5 ? 32 : 96 + 8 * (cg - 6));
    const int c1 = cg < 5 ? 48 + 8 * cg : 112 + 8 * (cg - 5);
    wo0 = (8 * hh + q) * 144 + c0 + odd; wo1 = (8 * hh + q) * 144 + c1 + odd; }

  f32x16 T0 = {}, T1 = {}, AB = {}, AB2 = {};
  float z = 0.f;
  int slot = 0;
  for (int kb = 0; kb < 10; ++kb) {
    if (kb == 0) WAIT_BAR(12); else if (kb < 9) WAIT_BAR(6); else WAIT_BAR(0);
    if (kb >= 1 && kb + 2 < 10) issue(kb + 2, slot == 0 ? 2 : slot - 1);
    const LAS char* sb = lds + slot * K1_STAGE;
#pragma unroll
    for (int u = 0; u < 4; ++u) {
      const bf16x8 xf = ldsr128(sb + K1_X + w * 4096 + (ra ^ (u * 32)));
      const bf16x8 w0 = cat(ldstr(sb + K1_W + wo0 + u * 16 * 144), ldstr(sb + K1_W + wo0 + (u * 16 + 4) * 144));
      const bf16x8 w1 = cat(ldstr(sb + K1_W + wo1 + u * 16 * 144), ldstr(sb + K1_W + wo1 + (u * 16 + 4) * 144));
      const bf16x8 hf = ldsr128(sb + K1_H + (ra ^ (u * 32)));
      T0 = MFMA32(w0, xf, T0);
      T1 = MFMA32(w1, xf, T1);
      AB = MFMA32(xf, hf, AB);
      if (extra) { const bf16x8 h2 = ldsr128(sb + K1_H + 4096 + (ra ^ (u * 32))); AB2 = MFMA32(xf, h2, AB2); }
      if (gate) {
        const u32x4 gf = __builtin_bit_cast(u32x4, ldsr128(lds + K1_GW + kb * 128 + u * 32 + hh * 16));
        const u32x4 xu = __builtin_bit_cast(u32x4, xf);
        z = dot2(xu[0], gf[0], z); z = dot2(xu[1], gf[1], z); z = dot2(xu[2], gf[2], z); z = dot2(xu[3], gf[3], z);
      }
    }
    slot = slot == 2 ? 0 : slot + 1;
  }

  const int qt = j * 8 + w;
  const float cq = 1.4426950408889634f * 0.22360679774997896f;
  {
    const u32x4 q1 = {pk(T0[0] * cq, T0[1] * cq), pk(T0[2] * cq, T0[3] * cq), pk(T0[4] * cq, T0[5] * cq), pk(T0[6] * cq, T0[7] * cq)};
    const u32x2 q2 = {pk(T0[8] * cq, T0[9] * cq), pk(T0[10] * cq, T0[11] * cq)};
    *(u32x4*)(ws + WS_Q1 + (size_t)(h * 64 + qt) * 1024 + lane * 16) = q1;
    *(u32x2*)(ws + WS_Q2 + (size_t)(h * 64 + qt) * 512 + lane * 8) = q2;
  }
  char* kv = ws + WS_KV + (size_t)(h * 16 + (qt >> 2)) * KVG;
  const int t = w & 3;
  const u32x2 t1m = {pk(T1[8], T1[9]), pk(T1[10], T1[11])};
  {
    const u32x4 k1 = {pk(T1[0], T1[1]), pk(T1[2], T1[3]), pk(T1[4], T1[5]), pk(T1[6], T1[7])};
    *(u32x4*)(kv + t * 1024 + lane * 16) = k1;
    if (hh == 0) *(u32x2*)(kv + 4096 + (t >> 1) * 512 + r * 16 + (t & 1) * 8) = t1m;
    float n2 = dot2(k1[0], k1[0], 0.f); n2 = dot2(k1[1], k1[1], n2); n2 = dot2(k1[2], k1[2], n2); n2 = dot2(k1[3], k1[3], n2);
    if (hh == 0) { n2 = dot2(t1m[0], t1m[0], n2); n2 = dot2(t1m[1], t1m[1], n2); }
    n2 += __shfl_xor(n2, 32);
#pragma unroll
    for (int o = 16; o >= 1; o >>= 1) { const float y = __shfl_xor(n2, o); n2 = (y > n2 || y != y) ? y : n2; }
    if (lane == 0) *(float*)(ws + WS_KN + (size_t)(h * 64 + qt) * 4) = n2;
  }
  {
    LAS char* sc = (LAS char*)smem + K1_X + w * 4096;
    const u32x2 va = {pk(T0[12], T0[13]), pk(T0[14], T0[15])};
    const u32x2 vb = {pk(T1[12], T1[13]), pk(T1[14], T1[15])};
    const u32x2 one = {0x00003F80u, 0u};
    *(LAS u32x2*)(sc + r * 48 + hh * 8) = va;
    *(LAS u32x2*)(sc + r * 48 + 24 + hh * 8) = vb;
    *(LAS u32x2*)(sc + r * 48 + (hh ? 16 : 40)) = hh ? t1m : one;
    asm volatile("s_waitcnt lgkmcnt(0)" ::: "memory");
    const int db = (lane >> 4) & 1, q = (lane & 15) >> 2, p = lane & 3;
    const LAS char* rp = sc + (4 * hh + q) * 48 + db * 32 + p * 8;
#pragma unroll
    for (int s = 0; s < 2; ++s) {
      const bf16x8 vf = cat(ldstr(rp + s * 16 * 48), ldstr(rp + (s * 16 + 8) * 48));
      if (r <= 20) *(bf16x8*)(kv + 5120 + ((t * 2 + s) * 2 + hh) * 336 + r * 16) = vf;
    }
  }
  auto store_ab = [&](const f32x16& A, int et) {
#pragma unroll
    for (int s = 0; s < 2; ++s) {
      const unsigned d0 = pk(A[8 * s], A[8 * s + 1]), d1 = pk(A[8 * s + 2], A[8 * s + 3]), d2 = pk(A[8 * s + 4], A[8 * s + 5]), d3 = pk(A[8 * s + 6], A[8 * s + 7]);
      const auto s0 = __builtin_amdgcn_permlane32_swap(d0, d2, false, false);
      const auto s1 = __builtin_amdgcn_permlane32_swap(d1, d3, false, false);
      const u32x4 o = {s0[0], s1[0], s0[1], s1[1]};
      *(u32x4*)(ws + WS_AB + (size_t)(et * 128 + qt * 2 + s) * 1024 + lane * 16) = o;
    }
  };
  store_ab(AB, et0);
  if (extra) store_ab(AB2, et1);
  if (gate) {
    z += __shfl_xor(z, 32);
    if (hh == 0) *(float*)(ws + WS_G + (size_t)(qt * 32 + r) * 4) = 1.f / (1.f + __expf(-z));
  }
}

constexpr int K2_GST = 28672, K2_C = 0, K2_A = 8192, K2_KV = 4 * K2_GST, K2_LDS = K2_KV + 4 * KVG;

DI void attn_slow(const char* kvh, const bf16x8 q1, const u32x2 q2, int lane, f32x16& O) {
  const int r = lane & 31, hh = lane >> 5;
  float m = -INFINITY;
  O = f32x16{};
  const u32x4 qa = {hh ? 0u : q2[0], hh ? 0u : q2[1], 0u, 0u};
  for (int t = 0; t < 64; ++t) {
    const char* g = kvh + (size_t)(t >> 2) * KVG;
    const int tt = t & 3;
    const bf16x8 k1 = *(const bf16x8*)(g + tt * 1024 + lane * 16);
    const u32x2 k2 = *(const u32x2*)(g + 4096 + (tt >> 1) * 512 + r * 16 + (tt & 1) * 8);
    const u32x4 ka = {k2[0], k2[1], 0u, 0u};
    const int rv = r <= 20 ? r : 20;
    const bf16x8 v0 = *(const bf16x8*)(g + 5120 + ((tt * 2 + 0) * 2 + hh) * 336 + rv * 16);
    const bf16x8 v1 = *(const bf16x8*)(g + 5120 + ((tt * 2 + 1) * 2 + hh) * 336 + rv * 16);
    f32x16 S = MFMA32(k1, q1, f32x16{});
    S = MFMA32(asfrag(ka), asfrag(qa), S);
    float tm = S[0];
#pragma unroll
    for (int i = 1; i < 16; ++i) tm = fmaxf(tm, S[i]);
    tm = fmaxf(tm, __shfl_xor(tm, 32));
    const float mn = fmaxf(m, tm);
    const float al = __builtin_amdgcn_exp2f(m - mn);
    m = mn;
#pragma unroll
    for (int i = 0; i < 16; ++i) { O[i] *= al; S[i] = __builtin_amdgcn_exp2f(S[i] - mn); }
    const u32x4 p0 = {pk(S[0], S[1]), pk(S[2], S[3]), pk(S[4], S[5]), pk(S[6], S[7])};
    const u32x4 p1 = {pk(S[8], S[9]), pk(S[10], S[11]), pk(S[12], S[13]), pk(S[14], S[15])};
    O = MFMA32(v0, asfrag(p0), O);
    O = MFMA32(v1, asfrag(p1), O);
  }
}

__global__ __launch_bounds__(768, 3) void k2_main(const u16* __restrict__ cr, const u16* __restrict__ ci, char* __restrict__ ws) {
  __shared__ __attribute__((aligned(16))) char smem[K2_LDS];
  const LAS char* lds = (const LAS char*)smem;
  const unsigned ldsb = (unsigned)(size_t)lds;
  const int tid = threadIdx.x, lane = tid & 63, r = lane & 31, hh = lane >> 5;
  const int w = rfl(tid >> 6);
  const int bid = blockIdx.x;

  if (w < 8) {
    const int h = (bid & 7) * 4 + ((bid >> 3) & 3), j = bid >> 5, qt = j * 8 + w;
    const char* kvh = ws + WS_KV + (size_t)h * 16 * KVG;
    const rsrc_t rsKV = mkrs(kvh, 16 * KVG);
    const unsigned vl = lane * 16;
    const bf16x8 q1 = *(const bf16x8*)(ws + WS_Q1 + (size_t)(h * 64 + qt) * 1024 + lane * 16);
    const u32x2 q2 = *(const u32x2*)(ws + WS_Q2 + (size_t)(h * 64 + qt) * 512 + lane * 8);
    float kn = *(const float*)(ws + WS_KN + (size_t)(h * 64 + lane) * 4);
#pragma unroll
    for (int g = 0; g < 3; ++g) {
      dma16<0>(ldsb + K2_KV + g * KVG + w * 1024, vl, rsKV, g * KVG + w * 1024);
      if (w < 3) dma16<0>(ldsb + K2_KV + g * KVG + (8 + w) * 1024, vl, rsKV, g * KVG + (8 + w) * 1024);
    }
#pragma unroll
    for (int o = 32; o >= 1; o >>= 1) { const float y = __shfl_xor(kn, o); kn = (y > kn || y != y) ? y : kn; }
    const u32x4 q1u = __builtin_bit_cast(u32x4, q1);
    float qn = dot2(q1u[0], q1u[0], 0.f); qn = dot2(q1u[1], q1u[1], qn); qn = dot2(q1u[2], q1u[2], qn); qn = dot2(q1u[3], q1u[3], qn);
    if (hh == 0) { qn = dot2(q2[0], q2[0], qn); qn = dot2(q2[1], q2[1], qn); }
    qn += __shfl_xor(qn, 32);
    const float mb = sqrtf(qn) * sqrtf(kn);
    f32x16 CI;
#pragma unroll
    for (int i = 0; i < 16; ++i) CI[i] = -mb;
    const unsigned z0 = hh ? 0u : q2[0], z1 = hh ? 0u : q2[1], y0 = hh ? q2[0] : 0u, y1 = hh ? q2[1] : 0u;
    const bf16x8 qs0 = asfrag(u32x4{z0, z1, 0u, 0u}), qs1 = asfrag(u32x4{0u, 0u, z0, z1});
    const bf16x8 qs2 = asfrag(u32x4{y0, y1, 0u, 0u}), qs3 = asfrag(u32x4{0u, 0u, y0, y1});
    f32x16 O = {};
    const int voff = 5120 + hh * 336 + r * 16;
    auto tile = [&](const LAS char* g, const bf16x8& k2, const bf16x8& qs, int t) {
      const bf16x8 k1 = ldsr128(g + t * 1024 + lane * 16);
      const bf16x8 v0 = ldsr128(g + voff + (t * 2) * 672);
      const bf16x8 v1 = ldsr128(g + voff + (t * 2 + 1) * 672);
      f32x16 S = MFMA32(k1, q1, CI);
      S = MFMA32(k2, qs, S);
#pragma unroll
      for (int i = 0; i < 16; ++i) S[i] = __builtin_amdgcn_exp2f(S[i]);
      const u32x4 p0 = {pk(S[0], S[1]), pk(S[2], S[3]), pk(S[4], S[5]), pk(S[6], S[7])};
      const u32x4 p1 = {pk(S[8], S[9]), pk(S[10], S[11]), pk(S[12], S[13]), pk(S[14], S[15])};
      O = MFMA32(v0, asfrag(p0), O);
      O = MFMA32(v1, asfrag(p1), O);
    };
    for (int G = 0; G < 16; ++G) {
      const LAS char* g = lds + K2_KV + (G & 3) * KVG;
      const unsigned nslot = ldsb + K2_KV + ((G + 3) & 3) * KVG;
      if (G == 0) WAIT_BAR(0); else WAIT_BAR(2);
      if (G + 3 < 16) dma16<0>(nslot + w * 1024, vl, rsKV, (G + 3) * KVG + w * 1024);
      const bf16x8 k2 = ldsr128(g + 4096 + lane * 16);
      tile(g, k2, qs0, 0);
      tile(g, k2, qs1, 1);
      WAIT_BAR(2);
      if (G + 3 < 16 && w < 3) dma16<0>(nslot + (8 + w) * 1024, vl, rsKV, (G + 3) * KVG + (8 + w) * 1024);
      tile(g, k2, qs2, 2);
      tile(g, k2, qs3, 3);
      if (G >= 12) asm volatile("s_waitcnt vmcnt(0)" ::: "memory");
    }
    float l = __shfl(O[8], 32 + r);
    if (__any(!(l >= 8.271806125530277e-25f))) {
      attn_slow(kvh, q1, q2, lane, O);
      l = __shfl(O[8], 32 + r);
    }
    const float il = 1.f / l;
    char* ob = ws + WS_O + (size_t)qt * 40 * 1024 + r * 16;
#pragma unroll
    for (int g = 0; g < 3; ++g) {
      const int k0 = 20 * h + 8 * g + 4 * hh;
      const u32x2 ov = {pk(O[4 * g] * il, O[4 * g + 1] * il), pk(O[4 * g + 2] * il, O[4 * g + 3] * il)};
      if (g < 2 || hh == 0) *(u32x2*)(ob + (k0 >> 3) * 512 + ((((k0 >> 2) ^ (k0 >> 3)) & 1) << 3)) = ov;
    }
  } else {
    const int gwv = w - 8, m = gwv & 1, kh = gwv >> 1;
    const int xcd = bid & 7, idx = bid >> 3, part = xcd >> 2, mt = (xcd & 3) * 8 + (idx >> 2), nt = idx & 3;
    const u16* cm = (part ? ci : cr) + (size_t)(mt * 64 + gwv * 16) * SEQ;
    const rsrc_t rsC = mkrs(cm, 16 * SEQ * 2);
    const rsrc_t rsA = mkrs(ws + WS_AB + (size_t)(part * 20 + nt * 5) * 128 * 1024, 5 * 128 * 1024);
    const unsigned vc0 = (lane >> 3) * 4096 + (((lane & 7) ^ (lane >> 4)) << 4);
    const unsigned vc1 = (vc0 ^ 64) + 8 * 4096;
    const unsigned vl = lane * 16;
    auto issue = [&](int e) {
      const unsigned sb = ldsb + (e & 3) * K2_GST;
      dma16<0>(sb + K2_C + gwv * 2048, vc0, rsC, e * 128);
      dma16<0>(sb + K2_C + gwv * 2048 + 1024, vc1, rsC, e * 128);
#pragma unroll
      for (int i = 0; i < 5; ++i) dma16<0>(sb + K2_A + (i * 4 + gwv) * 1024, vl, rsA, (i * 128 + e * 4 + gwv) * 1024);
    };
    issue(0); issue(1); issue(2);
    const int f = (r >> 1) & 7;
    const int ra = (m * 32 + r) * 128 + (((hh ^ f) ^ (kh * 4)) << 4);
    f32x16 acc[5] = {};
    for (int e = 0; e < 32; ++e) {
      if (e < 30) WAIT_BAR(14); else if (e == 30) WAIT_BAR(7); else WAIT_BAR(0);
      if (e + 3 < 32) issue(e + 3);
      const LAS char* sb = lds + (e & 3) * K2_GST;
#pragma unroll
      for (int u = 0; u < 2; ++u) {
        const bf16x8 cf = ldsr128(sb + K2_C + (ra ^ (u * 32)));
#pragma unroll
        for (int i = 0; i < 5; ++i) {
          const bf16x8 af = ldsr128(sb + K2_A + (i * 4 + kh * 2 + u) * 1024 + lane * 16);
          acc[i] = MFMA32(af, cf, acc[i]);
        }
      }
    }
    LAS char* red = (LAS char*)smem + m * 20480 + lane * 16;
    if (kh == 1) {
#pragma unroll
      for (int i = 0; i < 5; ++i)
#pragma unroll
        for (int g = 0; g < 4; ++g) *(LAS f32x4*)(red + (i * 4 + g) * 1024) = f32x4{acc[i][4 * g], acc[i][4 * g + 1], acc[i][4 * g + 2], acc[i][4 * g + 3]};
    }
    WAIT_BAR(0);
    if (kh == 0) {
      char* yb = ws + WS_Y + ((size_t)(part * 64 + mt * 2 + m) * 160 + nt * 40) * 256 + lane * 8;
#pragma unroll
      for (int i = 0; i < 5; ++i)
#pragma unroll
        for (int g = 0; g < 4; ++g) {
          const f32x4 o = *(const LAS f32x4*)(red + (i * 4 + g) * 1024);
          const u32x2 yv = {pk(acc[i][4 * g] + o[0], acc[i][4 * g + 1] + o[1]), pk(acc[i][4 * g + 2] + o[2], acc[i][4 * g + 3] + o[3])};
          *(u32x2*)(yb + (i * 8 + g * 2) * 256) = yv;
        }
    }
  }
}

constexpr int K3_LDS = 640 * 160 + 4 * 5 * 1024;

__global__ __launch_bounds__(512, 2) void k3_out(const u16* __restrict__ wo, const char* __restrict__ ws, u16* __restrict__ out) {
  __shared__ __attribute__((aligned(16))) char smem[K3_LDS];
  const LAS char* lds = (const LAS char*)smem;
  const unsigned ldsb = (unsigned)(size_t)lds;
  const int tid = threadIdx.x, lane = tid & 63, t16 = lane & 15, kq = lane >> 4;
  const int w = rfl(tid >> 6), m = w & 3, kh = w >> 2;
  const int bid = blockIdx.x, mt = (bid & 7) * 4 + ((bid >> 3) & 3), nt = bid >> 5;
  const rsrc_t rsW = mkrs((const char*)wo + nt * 160, DM * DM * 2 - nt * 160);
  unsigned vw[5];
#pragma unroll
  for (int b = 0; b < 5; ++b) { const int g = b * 64 + lane; vw[b] = (g / 10) * 1280 + (g % 10) * 16; }
#pragma unroll
  for (int a3 = 0; a3 < 3; ++a3) {
    const int a = w + 8 * a3;
    if (a < 20) {
#pragma unroll
      for (int b = 0; b < 5; ++b) dma16<0>(ldsb + (a * 5 + b) * 1024, vw[b], rsW, a * 32 * 1280);
    }
  }
  const int tt = mt * 2 + (m >> 1), t32 = (m & 1) * 16 + t16;
  const char* op = ws + WS_O + (size_t)tt * 40 * 1024 + (kh * 20 * 1024) + kq * 512 + t32 * 16;
  bf16x8 of[10];
#pragma unroll
  for (int v = 0; v < 10; ++v) of[v] = *(const bf16x8*)(op + v * 2048);
  u32x2 yr[5], yi[5];
  float g = 0.f;
  if (kh == 0) {
    const char* yp = ws + WS_Y + ((size_t)tt * 160 + nt * 20 + kq) * 256 + t32 * 8;
#pragma unroll
    for (int i = 0; i < 5; ++i) { yr[i] = *(const u32x2*)(yp + i * 1024); yi[i] = *(const u32x2*)(yp + (size_t)64 * 160 * 256 + i * 1024); }
    g = *(const float*)(ws + WS_G + (size_t)(tt * 32 + t32) * 4);
  }
  WAIT_BAR(0);
  f32x4 acc[5] = {};
  const int q = (lane & 15) >> 2, p = lane & 3;
  const LAS char* wp0 = lds + (kh * 320 + 8 * kq + 4 * (kq & 1) + q) * 160 + p * 8;
  const LAS char* wp1 = lds + (kh * 320 + 8 * kq + 4 * ((kq & 1) ^ 1) + q) * 160 + p * 8;
#pragma unroll
  for (int v = 0; v < 10; ++v)
#pragma unroll
    for (int i = 0; i < 5; ++i) {
      const bf16x8 wf = cat(ldstr(wp0 + v * 32 * 160 + i * 32), ldstr(wp1 + v * 32 * 160 + i * 32));
      acc[i] = MFMA16(wf, of[v], acc[i]);
    }
  LAS char* red = (LAS char*)smem + 640 * 160 + m * 5120 + lane * 16;
  if (kh == 1) {
#pragma unroll
    for (int i = 0; i < 5; ++i) *(LAS f32x4*)(red + i * 1024) = acc[i];
  }
  WAIT_BAR(0);
  if (kh == 0) {
    u16* orow = out + (size_t)(tt * 32 + t32) * DM + nt * 80 + 4 * kq;
    const float og = 1.f - g;
#pragma unroll
    for (int i = 0; i < 5; ++i) {
      const f32x4 o = *(const LAS f32x4*)(red + i * 1024) + acc[i];
      const float y0 = __uint_as_float(yr[i][0] << 16) - __uint_as_float(yi[i][0] << 16);
      const float y1 = __uint_as_float(yr[i][0] & 0xffff0000u) - __uint_as_float(yi[i][0] & 0xffff0000u);
      const float y2 = __uint_as_float(yr[i][1] << 16) - __uint_as_float(yi[i][1] << 16);
      const float y3 = __uint_as_float(yr[i][1] & 0xffff0000u) - __uint_as_float(yi[i][1] & 0xffff0000u);
      const u32x2 ov = {pk(g * y0 + og * o[0], g * y1 + og * o[1]), pk(g * y2 + og * o[2], g * y3 + og * o[3])};
      *(u32x2*)(orow + i * 16) = ov;
    }
  }
}

extern "C" void kernel_launch(void* const* d_in, const int* in_sizes, int n_in, void* d_out, int out_size, void* d_ws, size_t ws_size,
                              hipStream_t stream) {
  const u16* x = (const u16*)d_in[0];
  const u16* cr = (const u16*)d_in[1];
  const u16* ci = (const u16*)d_in[2];
  const u16* hr = (const u16*)d_in[3];
  const u16* hi = (const u16*)d_in[4];
  const u16* wqkv = (const u16*)d_in[6];
  const u16* wo = (const u16*)d_in[7];
  const u16* gw = (const u16*)d_in[8];
  char* ws = (char*)d_ws;
  k1_proj<<<256, 512, 0, stream>>>(x, hr, hi, wqkv, gw, ws);
  k2_main<<<256, 768, 0, stream>>>(cr, ci, ws);
  k3_out<<<256, 512, 0, stream>>>(wo, ws, (u16*)d_out);
}
```

```cpp
#include <hip/hip_runtime.h>
#include <hip/hip_bf16.h>

typedef unsigned short u16;
typedef __attribute__((ext_vector_type(8))) short bf16x8;
typedef __attribute__((ext_vector_type(4))) short s16x4;
typedef __attribute__((ext_vector_type(16))) float f32x16;
typedef __attribute__((ext_vector_type(4))) float f32x4;
typedef __attribute__((ext_vector_type(2))) float f32x2;
typedef __attribute__((ext_vector_type(4))) unsigned u32x4;
typedef __attribute__((ext_vector_type(2))) unsigned u32x2;
typedef __attribute__((ext_vector_type(2))) __bf16 bf16x2_t;
typedef __amdgpu_buffer_rsrc_t rsrc_t;
#define LAS __attribute__((address_space(3)))
#define DI __device__ __forceinline__
#define MFMA32(a, b, c) __builtin_amdgcn_mfma_f32_32x32x16_bf16((a), (b), (c), 0, 0, 0)
#define MFMA16(a, b, c) __builtin_amdgcn_mfma_f32_16x16x32_bf16((a), (b), (c), 0, 0, 0)

constexpr int SEQ = 2048, DM = 640;
constexpr size_t WS_Q = 0;
constexpr int KV_K2 = 4096, KV_V = 6144, KV_VT = 1408, KVG = 12288;
constexpr size_t WS_KV = WS_Q + 32 * 64 * 1280;
constexpr size_t WS_AB = WS_KV + (size_t)32 * 16 * KVG;
constexpr size_t WS_G = WS_AB + (size_t)40 * 128 * 1024;
constexpr size_t WS_O = WS_G + 2048 * 4;
constexpr size_t WS_Y = WS_O + (size_t)64 * 40 * 1024;
constexpr size_t YPART = (size_t)64 * 160 * 256;
constexpr size_t WS_FLAG = WS_Y + 4 * YPART;
constexpr size_t WS_FLAG1 = WS_FLAG + 32 * 512;
constexpr size_t WS_FLAG2 = WS_FLAG1 + 16 * 256;
constexpr size_t WS_CNT = WS_FLAG2 + 8 * 256;
constexpr int C_Y = 0, C_O = 64, C_NUM = 96;
constexpr size_t WS_EPOCH = WS_CNT + C_NUM * 256;
constexpr size_t WS_END = WS_EPOCH + 256;

#define SBAR() __builtin_amdgcn_sched_barrier(0)
#define PIN(x) asm volatile("" : "+v"(x))
DI unsigned rfl(unsigned x) { return __builtin_amdgcn_readfirstlane(x); }
DI unsigned pk(float lo, float hi) {
  f32x2 v = {lo, hi};
  bf16x2_t b = __builtin_convertvector(v, bf16x2_t);
  return __builtin_bit_cast(unsigned, b);
}
DI float dot2(unsigned a, unsigned b, float c) {
  return __builtin_amdgcn_fdot2_f32_bf16(__builtin_bit_cast(bf16x2_t, a), __builtin_bit_cast(bf16x2_t, b), c, false);
}
DI rsrc_t mkrs(const void* p, unsigned bytes) { return __builtin_amdgcn_make_buffer_rsrc((void*)p, (short)0, (int)bytes, 0x00020000); }
template <int OFF>
DI void dma16(unsigned m0v, unsigned voff, rsrc_t rs, unsigned soff) {
  asm volatile("s_mov_b32 m0, %0\n\ts_nop 0\n\tbuffer_load_dwordx4 %1, %2, %3 offen offset:%4 lds"
               :: "s"(m0v), "v"(voff), "s"(rs), "s"(soff), "i"(OFF) : "memory");
}
template <int OFF>
DI void dma16c(unsigned m0v, unsigned voff, rsrc_t rs, unsigned soff) {
  asm volatile("s_mov_b32 m0, %0\n\ts_nop 0\n\tbuffer_load_dwordx4 %1, %2, %3 offen offset:%4 sc1 lds"
               :: "s"(m0v), "v"(voff), "s"(rs), "s"(soff), "i"(OFF) : "memory");
}
constexpr unsigned long long vmask(int piece) {
  unsigned long long m = 0;
  for (int l = 0; l < 64; ++l) { const int o = piece * 1024 + l * 16 - KV_V; if (o < 4 * KV_VT && o % KV_VT < 1280) m |= 1ull << l; }
  return m;
}
template <int OFF, int PIECE>
DI void dma16m(unsigned m0v, unsigned voff, rsrc_t rs, unsigned soff) {
  constexpr int lo = (int)(unsigned)(vmask(PIECE) & 0xffffffffull), hi = (int)(unsigned)(vmask(PIECE) >> 32);
  asm volatile("s_mov_b32 m0, %0\n\ts_mov_b32 exec_lo, %5\n\ts_mov_b32 exec_hi, %6\n\tbuffer_load_dwordx4 %1, %2, %3 offen offset:%4 sc1 lds\n\ts_mov_b64 exec, -1"
               :: "s"(m0v), "v"(voff), "s"(rs), "s"(soff), "i"(OFF), "i"(lo), "i"(hi) : "memory");
}
DI void touch(unsigned m0v, unsigned voff, rsrc_t rs) {
  asm volatile("s_mov_b32 m0, %0\n\ts_nop 0\n\tbuffer_load_dword %1, %2, 0 offen sc1 lds" :: "s"(m0v), "v"(voff), "s"(rs) : "memory");
}
DI unsigned x32(unsigned v, int lane) { const auto s = __builtin_amdgcn_permlane32_swap(v, v, false, false); return (lane & 32) ? s[0] : s[1]; }
DI float hi32f(float v) { return __uint_as_float(__builtin_amdgcn_permlane32_swap(__float_as_uint(v), __float_as_uint(v), false, false)[1]); }
DI float lo32f(float v) { return __uint_as_float(__builtin_amdgcn_permlane32_swap(__float_as_uint(v), __float_as_uint(v), false, false)[0]); }
DI float x32f(float v, int lane) { return __uint_as_float(x32(__float_as_uint(v), lane)); }
#define WAIT_BAR(N) asm volatile("s_waitcnt vmcnt(" #N ") lgkmcnt(0)\n\ts_barrier" ::: "memory")
DI bf16x8 ldsr128(const LAS char* p) { return *(const LAS bf16x8*)p; }
DI s16x4 ldstr(const LAS char* p) { return __builtin_amdgcn_ds_read_tr16_b64_v4i16((LAS s16x4*)p); }
DI bf16x8 cat(s16x4 a, s16x4 b) { return __builtin_shufflevector(a, b, 0, 1, 2, 3, 4, 5, 6, 7); }
DI bf16x8 asfrag(u32x4 v) { return __builtin_bit_cast(bf16x8, v); }

#define POLL_SLP 2
#define SLP_LAZY 6
#define SLP 1
#define G_BOOST 6
#define X_CODE 24576
#define PRIO_G 1
#define PRIO_A 2
constexpr int PS = 43008, R_H = 32768;
constexpr int RS = 36864, R_X = 0, R_B = 16384, L_KV = 3 * RS, L_PROG = L_KV + 4 * KVG, L_GATE = L_PROG + 128, L_SCR = L_GATE + 1024, L_EP = L_SCR + 256, L_TOTAL = L_EP + 256;
constexpr int P_CONS = 0;
constexpr int P_LAND = 8;
constexpr int P_KVL = 23;
constexpr int P_KVC = 12;
constexpr int P_CNT = 16;
constexpr int P_EP = (L_EP - L_PROG) / 4, P_TAGV = 21, P_GO = 22;
constexpr int P_GOQ = 24, P_KV0 = 28;
constexpr int P_WO = 25;

DI void attn_slow(const char* kvh, const bf16x8 q1, const u32x2 q2, int odd, int lane, f32x16& O) {
  const int r = lane & 31, hh = lane >> 5;
  const rsrc_t rs = mkrs(kvh, 16 * KVG);
  float m = -INFINITY;
  O = f32x16{};
  const u32x4 qa = {hh ? 0u : q2[0], hh ? 0u : q2[1], 0u, 0u};
#pragma unroll 1
  for (int t = 0; t < 64; ++t) {
    const int tt = t & 3, g = (t >> 2) * KVG;
    const bf16x8 k1 = asfrag(__builtin_amdgcn_raw_buffer_load_b128(rs, lane * 16, g + tt * 1024, 16));
    const u32x2 k2 = __builtin_amdgcn_raw_buffer_load_b64(rs, r * 16 + odd * 8, g + KV_K2 + tt * 512, 16);
    const u32x4 ka = {k2[0], k2[1], 0u, 0u};
    const unsigned rv = r < 20 ? r * 64 + ((hh ^ ((r >> 2) & 3)) << 4) : 0x80000000u, one = r == 20 ? 0x3F803F80u : 0u;
    const bf16x8 v0 = asfrag(__builtin_amdgcn_raw_buffer_load_b128(rs, rv, g + KV_V + tt * KV_VT, 16) | one);
    const bf16x8 v1 = asfrag(__builtin_amdgcn_raw_buffer_load_b128(rs, rv ^ 32, g + KV_V + tt * KV_VT, 16) | one);
    f32x16 S = MFMA32(k1, q1, f32x16{});
    S = MFMA32(asfrag(ka), asfrag(qa), S);
    float tm = S[0];
#pragma unroll
    for (int i = 1; i < 16; ++i) tm = fmaxf(tm, S[i]);
    tm = fmaxf(tm, __shfl_xor(tm, 32));
    const float mn = fmaxf(m, tm);
    const float al = __builtin_amdgcn_exp2f(m - mn);
    m = mn;
#pragma unroll
    for (int i = 0; i < 16; ++i) { O[i] *= al; S[i] = __builtin_amdgcn_exp2f(S[i] - mn); }
    const u32x4 p0 = {pk(S[0], S[1]), pk(S[2], S[3]), pk(S[4], S[5]), pk(S[6], S[7])};
    const u32x4 p1 = {pk(S[8], S[9]), pk(S[10], S[11]), pk(S[12], S[13]), pk(S[14], S[15])};
    O = MFMA32(v0, asfrag(p0), O);
    O = MFMA32(v1, asfrag(p1), O);
  }
}

#define GAS __attribute__((address_space(1)))
#define SPIN_N(cond, n) do { _Pragma("unroll 1") for (int it_ = 0; it_ < (1 << 20); ++it_) { if (cond) break; __builtin_amdgcn_s_sleep(n); } \
    asm volatile("" ::: "memory"); } while (0)
#define SPIN_UNTIL(cond) SPIN_N(cond, SLP)
#define SPIN_LAZY(cond) SPIN_N(cond, SLP_LAZY)
typedef volatile LAS int* prog_t;
DI unsigned get_tag(prog_t prog) { SPIN_UNTIL(prog[P_TAGV] != 0); return rfl(prog[P_EP]) + 1u; }
DI bool drain_last(prog_t prog, int ph, int n, int lane) {
  asm volatile("s_waitcnt vmcnt(0)" ::: "memory");
  int old = 0;
  if (lane == 0) old = __hip_atomic_fetch_add((LAS int*)(prog + P_CNT + ph), 1, __ATOMIC_RELAXED, __HIP_MEMORY_SCOPE_WORKGROUP);
  return rfl(old) == n - 1;
}
DI void publish0(prog_t prog, char* ws, int cb, int rb, int lane) {
  const unsigned tag = get_tag(prog);
  if (drain_last(prog, 0, 4, lane)) {
    const int ty = cb / 5, cc = cb - ty * 5, hd = lane >> 1, wh = lane & 1;
    GAS unsigned* f = (GAS unsigned*)(ws + (cb == 15 ? WS_FLAG2 + lane * 256 + rb * 4 : WS_FLAG + hd * 512 + (ty ? rb * 16 + (ty - 1) * 8 : 256 + rb * 8) + wh * 4));
    if (cb == 15 ? lane < 8 : cc == (20 * hd + 19 * wh) >> 7) __hip_atomic_store(f, tag, __ATOMIC_RELAXED, __HIP_MEMORY_SCOPE_AGENT);
  }
}
DI void await_flags(prog_t prog, char* p, bool need, int lane) {
  const unsigned tag = get_tag(prog);
  const rsrc_t rs = mkrs(p, 512);
  const unsigned off = need ? lane * 16 : 0x80000000u;
#pragma unroll 1
  for (int it = 0; it < (1 << 18); ++it) {
    asm volatile("" ::: "memory");
    const u32x4 f = __builtin_amdgcn_raw_buffer_load_b128(rs, off, 0, 16);
    if (__all(!need || (f[0] == tag && f[1] == tag && f[2] == tag && f[3] == tag))) break;
    __builtin_amdgcn_s_sleep(POLL_SLP);
  }
  __builtin_amdgcn_fence(__ATOMIC_ACQUIRE, "wavefront");
}
DI void publish(prog_t prog, int ph, char* ws, int c, int lane) {
  SPIN_UNTIL(prog[P_GO] != 0);
  if (drain_last(prog, ph, 4, lane) && lane == 0)
    __hip_atomic_fetch_add((GAS unsigned*)(ws + WS_CNT + c * 256), 1u, __ATOMIC_RELAXED, __HIP_MEMORY_SCOPE_AGENT);
}
DI void await(prog_t prog, int ph, char* ws, int c, unsigned n) {
  SPIN_UNTIL(prog[P_CNT + ph] >= 4);
#pragma unroll 1
  for (int it = 0; it < (1 << 18); ++it) {
    if (__all(__hip_atomic_load((GAS unsigned*)(ws + WS_CNT + c * 256), __ATOMIC_RELAXED, __HIP_MEMORY_SCOPE_AGENT) >= n)) break;
    __builtin_amdgcn_s_sleep(POLL_SLP);
  }
  __builtin_amdgcn_fence(__ATOMIC_ACQUIRE, "wavefront");
}
#define ST64(v, rs, voff, soff) __builtin_amdgcn_raw_buffer_store_b64((v), (rs), (voff), (soff), 16)
#define ST128(v, rs, voff, soff) __builtin_amdgcn_raw_buffer_store_b128((v), (rs), (voff) + (unsigned)(soff), 0, 16)
#define LD64(rs, voff, soff) __builtin_amdgcn_raw_buffer_load_b64((rs), (voff), (soff), 16)
#define LD128(rs, voff, soff) __builtin_amdgcn_raw_buffer_load_b128((rs), (voff), (soff), 16)
DI u32x2 pk4(const f32x4& a, float s) { return u32x2{pk(a[0] * s, a[1] * s), pk(a[2] * s, a[3] * s)}; }
DI u32x2 pk4(const f32x4& a) { return u32x2{pk(a[0], a[1]), pk(a[2], a[3])}; }

DI u32x4 merge16(const u32x2 a, const u32x2 b) {
  const auto s0 = __builtin_amdgcn_permlane16_swap(a[0], b[0], false, false);
  const auto s1 = __builtin_amdgcn_permlane16_swap(a[1], b[1], false, false);
  return u32x4{s0[0], s1[0], s0[1], s1[1]};
}

template <bool SWAP>
DI void qkv_loop(const LAS char* lds, prog_t prog, int w, int lane, f32x4 (&acc)[4][4]) {
  const int c = lane & 15, g = lane >> 4;
  const int xoff = R_X + ((w & 1) * 64 + c) * 128 + ((g ^ ((c >> 1) & 7)) << 4);
  const int woff = R_B + (8 * g + (c >> 2)) * 256 + ((((c >> 2) | ((g & 1) << 2)) ^ ((w >> 1) * 4)) << 5) + (c & 3) * 8;
  const prog_t pl = prog + P_LAND + (lane & 3);
  bf16x8 xf[2][4], wf[2][4];
#define QRD(sb, u, n) do { if ((n) < 4) xf[u][n] = ldsr128((sb) + (xoff ^ ((u) * 64)) + (n) * 2048); \
    else { const LAS char* p_ = (sb) + (woff ^ (((n) - 4) * 32)) + (u) * 8192; wf[u][(n) - 4] = cat(ldstr(p_), ldstr(p_ + 1024)); } } while (0)
#define QMF(u, n) acc[(n) & 3][(n) >> 2] = SWAP ? MFMA16(wf[u][(n) >> 2], xf[u][(n) & 3], acc[(n) & 3][(n) >> 2]) : MFMA16(xf[u][(n) & 3], wf[u][(n) >> 2], acc[(n) & 3][(n) >> 2])
  SPIN_UNTIL(__all(*pl > 10));
#pragma unroll
  for (int n = 0; n < 8; ++n) QRD(lds, 0, n);
  int slot = 0;
#pragma unroll 1
  for (int t = 10; t < 20; ++t) {
    const LAS char* sb = lds + slot * PS;
    slot = slot == 2 ? 0 : slot + 1;
    const LAS char* sn = lds + slot * PS;
    int pv;
    SBAR();
#pragma unroll
    for (int n = 0; n < 16; ++n) {
      if (n < 8) QRD(sb, 1, n);
      if (n == 12) { asm volatile("s_waitcnt lgkmcnt(0)" ::: "memory"); prog[P_CONS + w] = t == 19 ? 1000 : t + 1; pv = *pl; }
      QMF(0, n); SBAR();
    }
    if (t < 19 && __builtin_expect(!__all(pv > t + 1), 0)) SPIN_UNTIL(__all(*pl > t + 1));
    asm volatile("" ::: "memory");
    SBAR();
#pragma unroll
    for (int n = 0; n < 16; ++n) { if (n < 8) QRD(sn, 0, n); QMF(1, n); SBAR(); }
  }
}

DI int red_off(int part, int tile) { return (part < 4 ? part * 20480 : 102400) + tile * 1024; }
DI void final_gemm(char* smem, prog_t prog, char* ws, int w, int lane, int fmt) {
  const LAS char* lds = (const LAS char*)smem;
  const int c = lane & 15, kq = lane >> 4, mh = w & 1, kp = w >> 1;
  bf16x8 wf[4][5];
  SPIN_LAZY(__all(prog[P_WO + (lane & 3) % 3] != 0));
  const LAS char* wp0 = lds + (8 * kq + 4 * (kq & 1) + (c >> 2)) * 160 + (lane & 3) * 8;
  const LAS char* wp1 = lds + (8 * kq + 4 * ((kq & 1) ^ 1) + (c >> 2)) * 160 + (lane & 3) * 8;
#pragma unroll
  for (int v = 0; v < 4; ++v)
#pragma unroll
    for (int i = 0; i < 5; ++i) wf[v][i] = cat(ldstr(wp0 + kp * 20480 + v * 5120 + i * 32), ldstr(wp1 + kp * 20480 + v * 5120 + i * 32));
  WAIT_BAR(0);
  await(prog, 3, ws, C_O + (fmt >> 2) * 4 + kp, 8);
  const rsrc_t rsO = mkrs(ws + WS_O + (size_t)(fmt * 2 + mh) * 40 * 1024, 40 * 1024);
  bf16x8 of[5][2];
#pragma unroll
  for (int v = 0; v < 4; ++v)
#pragma unroll
    for (int a = 0; a < 2; ++a) of[v][a] = asfrag(LD128(rsO, kq * 512 + (a * 16 + c) * 16, (kp * 4 + v) * 2048));
#pragma unroll
  for (int a = 0; a < 2; ++a) {
    const u32x2 x0 = LD64(rsO, kq * 512 + (a * 16 + c) * 8, (16 + kp) * 2048);
    const u32x2 x1 = LD64(rsO, kq * 512 + (a * 16 + c) * 8 + 256, (16 + kp) * 2048);
    of[4][a] = asfrag(u32x4{x0[0], x0[1], x1[0], x1[1]});
  }
  SBAR();
  f32x4 fac[2][5] = {};
#pragma unroll
  for (int v = 0; v < 5; ++v) {
#pragma unroll
    for (int a = 0; a < 2; ++a)
#pragma unroll
      for (int i = 0; i < 5; ++i) fac[a][i] = MFMA16(wf[v & 3][i], of[v][a], fac[a][i]);
    if (v == 0) {
      SBAR();
#pragma unroll
      for (int i = 0; i < 5; ++i) wf[0][i] = cat(ldstr(wp0 + (16 + kp) * 5120 + i * 32), ldstr(wp1 + (16 + kp) * 5120 + i * 32));
      SBAR();
    }
  }
#pragma unroll
  for (int a = 0; a < 2; ++a)
#pragma unroll
    for (int i = 0; i < 5; ++i) *(LAS f32x4*)((LAS char*)smem + red_off(kp, (mh * 2 + a) * 5 + i) + lane * 16) = fac[a][i];
}
DI void final_seed(char* smem, prog_t prog, char* ws, int rt, int lane, int fmt, int fnt) {
  await(prog, 2, ws, C_Y + (fmt >> 1) * 4 + (fnt >> 1), 4);
  const rsrc_t rsY = mkrs(ws + WS_Y, 4 * YPART);
  u32x2 yp4[4][5];
#pragma unroll
  for (int pp = 0; pp < 4; ++pp) {
    const unsigned yb = pp * YPART + (fmt * 8 + fnt) * 10240;
    const u32x4 a = LD128(rsY, lane * 16, yb + rt * 2048), b = LD128(rsY, lane * 16, yb + rt * 2048 + 1024);
    yp4[pp][0] = u32x2{a[0], a[1]}; yp4[pp][1] = u32x2{a[2], a[3]}; yp4[pp][2] = u32x2{b[0], b[1]}; yp4[pp][3] = u32x2{b[2], b[3]};
    yp4[pp][4] = LD64(rsY, lane * 16, yb + (8 + (rt >> 1)) * 1024 + (rt & 1) * 8);
  }
  const float gt = __hip_atomic_load((GAS float*)(ws + WS_G + (fmt * 64 + rt * 16 + (lane & 15)) * 4), __ATOMIC_RELAXED, __HIP_MEMORY_SCOPE_AGENT);
#define LO(c, k) __uint_as_float(yp4[c][i][k] << 16)
#define HI(c, k) __uint_as_float(yp4[c][i][k] & 0xffff0000u)
  f32x4 yv[5];
#pragma unroll
  for (int i = 0; i < 5; ++i) {
    yv[i] = f32x4{gt * ((LO(0, 0) + LO(1, 0)) - (LO(2, 0) + LO(3, 0))), gt * ((HI(0, 0) + HI(1, 0)) - (HI(2, 0) + HI(3, 0))),
                      gt * ((LO(0, 1) + LO(1, 1)) - (LO(2, 1) + LO(3, 1))), gt * ((HI(0, 1) + HI(1, 1)) - (HI(2, 1) + HI(3, 1)))};
    PIN(yv[i]);
  }
  __builtin_amdgcn_s_setprio(0);
  WAIT_BAR(0);
#pragma unroll
  for (int i = 0; i < 5; ++i) *(LAS f32x4*)((LAS char*)smem + red_off(4, rt * 5 + i) + lane * 16) = yv[i];
}

__global__ __launch_bounds__(768, 3) void fused_kernel(const u16* __restrict__ x, const u16* __restrict__ wqkv, const u16* __restrict__ hr,
                                                       const u16* __restrict__ hi, char* __restrict__ ws, const u16* __restrict__ gw,
                                                       const u16* __restrict__ cr, const u16* __restrict__ ci, const u16* __restrict__ wo,
                                                       u16* __restrict__ out) {
  __shared__ __attribute__((aligned(16))) char smem[L_TOTAL];
  const LAS char* lds = (const LAS char*)smem;
  const prog_t prog = (prog_t)((LAS char*)smem + L_PROG);
  const unsigned ldsb = (unsigned)(size_t)lds;
  const int tid = threadIdx.x, lane = tid & 63;
  const int w = rfl(tid >> 6);
  const int bid = blockIdx.x, xcd = bid & 7, idx = bid >> 3;
  asm volatile("" :: "s"(x), "s"(wqkv), "s"(hr), "s"(hi), "s"(ws), "s"(gw));
  const int rb = (xcd & 1) * 8 + (idx & 7), cb = (xcd >> 1) * 4 + (idx >> 3);
  const int h = xcd * 4 + (idx & 3), j = idx >> 2;
  const int part = xcd >> 2, kh = (xcd >> 1) & 1, mt = (xcd & 1) * 8 + (idx >> 2), nt = idx & 3;
  const int fmt = xcd * 4 + (idx & 3), fnt = idx >> 2;
  const bool misc = cb == 15;
  const rsrc_t rsO = mkrs(ws + WS_O, 64 * 40 * 1024);
  const char* kvh = ws + WS_KV + (size_t)h * 16 * KVG;
  if (tid < 32) prog[tid] = (misc && tid < 4) ? 1000 : ((tid == P_LAND + 2 || tid == P_LAND + 3) ? 11 : 0);

  if (w >= 8) {
      const int m = w - 8, c = lane & 15, g = lane >> 4;
      const unsigned swz = ((lane & 7) ^ (lane >> 4)) << 4;
      const prog_t pc = prog + P_CONS + (lane & 7);
      const int hf = m & 1;
      const rsrc_t rsX = mkrs(x + (size_t)(rb * 128 + hf * 64) * DM, 64 * DM * 2);
      const rsrc_t rsW = mkrs((const char*)wqkv + cb * 256 + hf * 32 * 3840, misc ? 0 : DM * 3840 - cb * 256 - hf * 32 * 3840);
      const rsrc_t rsH = mkrs((cb < 8 ? hr : hi) + (size_t)((cb & 7) * 80 + hf * 40) * DM, 40 * DM * 2);
      const unsigned vx0 = (lane >> 3) * 1280 + swz, vx1 = vx0 ^ 64;
      const unsigned vw0 = g * 3840 + ((c ^ (g << 1)) << 4), vw1 = vw0 ^ 128;
      const unsigned vh0 = hf ? vx1 : vx0, vh1 = hf ? vx0 : vx1;
      int slot = m >> 1;
      if (m >= 2) asm volatile("s_waitcnt lgkmcnt(0)\n\ts_barrier" ::: "memory");
#pragma unroll 1
      for (int s = m >> 1; s < 10; s += 2) {
        if (s >= 3) SPIN_UNTIL(__all(*pc >= s + 8));
        const unsigned sb = ldsb + slot * PS, so = s * 128;
#pragma unroll
        for (int n = 0; n < 8; ++n) dma16<0>(sb + R_X + hf * 8192 + n * 1024, (n & 1) ? vx1 : vx0, rsX, so + n * 8 * 1280);
#pragma unroll
        for (int n = 0; n < 8; ++n) dma16<0>(sb + R_B + hf * 8192 + n * 1024, (n & 2) ? vw1 : vw0, rsW, (s * 64 + n * 4) * 3840);
#pragma unroll
        for (int n = 0; n < 5; ++n) dma16<0>(sb + R_H + hf * 5120 + n * 1024, (n & 1) ? vh1 : vh0, rsH, so + n * 8 * 1280);
        if (s == 0) asm volatile("s_waitcnt lgkmcnt(0)\n\ts_barrier" ::: "memory");
        asm volatile("s_waitcnt vmcnt(0)" ::: "memory");
        prog[P_LAND + m] = (m == 0 && s == 8) ? 1000 : (s == 9 ? 20 : s + 12);
        slot = slot == 0 ? 2 : slot - 1;
      }
  }
  asm volatile("" ::: "memory");
  if (w < 8) {
    if (w == 0) touch(ldsb + L_EP, lane * 4, mkrs(ws + WS_EPOCH, 4));
    if (w == 2) {
      unsigned long long pc;
      asm volatile("s_getpc_b64 %0" : "=s"(pc));
      touch(ldsb + L_SCR, lane < 7 ? (idx * 7 + lane) * 128 : 0x80000000u, mkrs((const void*)(pc & ~127ull), X_CODE));
    }
    asm volatile("s_waitcnt lgkmcnt(0)\n\ts_barrier" ::: "memory");
    const int c = lane & 15, g = lane >> 4;
    if (w < 4 && !misc) {
      const int type = cb / 5, wcol = (cb - type * 5) * 2 + (w >> 1);
      f32x4 acc[4][4] = {};
      if (type < 2) qkv_loop<true>(lds, prog, w, lane, acc); else qkv_loop<false>(lds, prog, w, lane, acc);
      if (w == 0) { asm volatile("s_waitcnt vmcnt(0)" ::: "memory"); prog[P_TAGV] = 1; }
      const rsrc_t rsKV = mkrs(ws + WS_KV, 32 * 16 * KVG);
      const int wr = (w & 1) * 2, qt = rb * 4 + wr;
      if (type == 0) {
        const float cq = 1.4426950408889634f * 0.22360679774997896f;
        const rsrc_t rsQ = mkrs(ws + WS_Q, 32 * 64 * 1280);
#pragma unroll
        for (int i = 0; i < 4; ++i) {
          const int ch = wcol * 8 + i * 2 + (g >> 1), pr = (ch * 205) >> 10, cc = ch - pr * 5;
#pragma unroll
          for (int a = 0; a < 2; ++a)
            ST128(merge16(pk4(acc[2 * a][i], cq), pk4(acc[2 * a + 1][i], cq)), rsQ, pr * 64 * 2560 + cc * 512 + ((g & 1) * 16 + c) * 16 + a * 2560, qt * 2560);
        }
      } else if (type == 1) {
        const int rho = 16 * (c >> 3) + 8 * (g & 1) + (c & 7);
#pragma unroll
        for (int i = 0; i < 4; ++i) {
          const int ch = wcol * 8 + i * 2 + (g >> 1), pr = (ch * 205) >> 10, cc = ch - pr * 5;
          const bool sh = cc == 2;
          const unsigned oa = (pr * 2 + (cc > 2)) * 16 * KVG + rho * 16 + (sh ? KV_K2 + wr * 512 : wr * 1024 + (cc > 2 ? cc - 3 : cc) * 512);
#pragma unroll
          for (int a = 0; a < 2; ++a) {
            const u32x4 kv = merge16(pk4(acc[2 * a][i]), pk4(acc[2 * a + 1][i]));
            const unsigned o = oa + (sh ? a * 512 : a * 1024);
            ST128((u32x4{kv[0], kv[1], sh ? 0x3F80u : kv[2], sh ? 0u : kv[3]}), rsKV, o, rb * KVG);
            if (__any(sh)) ST128((u32x4{0x3F80u, 0u, kv[2], kv[3]}), rsKV, sh ? o + 16 * KVG : 0x80000000u, rb * KVG);
          }
        }
      } else {
#pragma unroll
        for (int i = 0; i < 4; ++i) {
          const int col = wcol * 64 + i * 16 + c, hd = (col * 3277) >> 16, d = col - hd * 20;
#pragma unroll
          for (int a = 0; a < 2; ++a) {
            const u32x2 lo = pk4(acc[2 * a][i]), hi2 = pk4(acc[2 * a + 1][i]);
            ST128((u32x4{lo[0], lo[1], hi2[0], hi2[1]}), rsKV, hd * 16 * KVG + KV_V + (wr + a) * KV_VT + d * 64 + ((g ^ ((d >> 2) & 3)) << 4), rb * KVG);
          }
        }
      }
      publish0(prog, ws, cb, rb, lane);
    } else if (w < 4) {
      if (w == 0) { asm volatile("s_waitcnt vmcnt(0)" ::: "memory"); prog[P_TAGV] = 1; }
      if (rb == 0 && w < 2 && w * 64 + lane < C_NUM)
        __hip_atomic_store((GAS unsigned*)(ws + WS_CNT + (w * 64 + lane) * 256), 0u, __ATOMIC_RELAXED, __HIP_MEMORY_SCOPE_AGENT);
      u32x4 gv[5];
#pragma unroll
      for (int i = 0; i < 5; ++i) gv[i] = ((const u32x4*)gw)[i * 16 + c];
#pragma unroll 2
      for (int it = 0; it < 8; ++it) {
        const int row = rb * 128 + w * 32 + it * 4 + g;
        float z = 0.f;
#pragma unroll
        for (int i = 0; i < 5; ++i) {
          const u32x4 a = ((const u32x4*)(x + (size_t)row * DM))[i * 16 + c];
          z = dot2(a[0], gv[i][0], z); z = dot2(a[1], gv[i][1], z); z = dot2(a[2], gv[i][2], z); z = dot2(a[3], gv[i][3], z);
        }
#pragma unroll
        for (int o = 8; o >= 1; o >>= 1) z += __shfl_xor(z, o);
        if (c == 0) __hip_atomic_store((GAS float*)(ws + WS_G + row * 4), 1.f / (1.f + __expf(-z)), __ATOMIC_RELAXED, __HIP_MEMORY_SCOPE_AGENT);
      }
      publish0(prog, ws, cb, rb, lane);
    }
  }

  if (w < 4) {
    const int r = lane & 31, hh = lane >> 5, qt0 = j * 8 + w * 2;
    __builtin_amdgcn_s_setprio(PRIO_A);
    if (w == 0) {
      await_flags(prog, ws + WS_FLAG + h * 512, lane == 16 + j, lane);
      prog[P_GOQ] = 1;
    } else SPIN_UNTIL(prog[P_GOQ] != 0);
    bf16x8 q1[2]; u32x2 q2[2];
    const rsrc_t rsQ = mkrs(ws + WS_Q + (size_t)((h >> 1) * 64 + qt0) * 2560, 2 * 2560);
#pragma unroll
    for (int a = 0; a < 2; ++a) { q1[a] = asfrag(LD128(rsQ, lane * 16, a * 2560 + (h & 1) * 1536)); q2[a] = LD64(rsQ, 1024 + r * 16, a * 2560 + (h & 1) * 8); }
    f32x16 Oa = {}, Ob = {};
    const f32x16 Z = {};
    const LAS char* kl = lds + L_KV + lane * 16;
    const LAS char* vl0 = lds + L_KV + KV_V + (r <= 21 ? r : 21) * 64 + ((hh ^ (r < 28 ? (r <= 21 ? r >> 2 : 5) & 3 : 3)) << 4);
    const LAS char* vl1 = (const LAS char*)((unsigned)(size_t)vl0 ^ 32);
    SPIN_UNTIL(prog[P_KV0] != 0);
    bf16x8 k1 = ldsr128(kl), k2 = ldsr128(kl + KV_K2);
    bf16x8 qs[2][2];
#pragma unroll
    for (int a = 0; a < 2; ++a) {
      u32x4 qx = (h & 1) ? u32x4{0u, 0u, q2[a][0], q2[a][1]} : u32x4{q2[a][0], q2[a][1], 0u, 0u};
      f32x16 S = MFMA32(k1, q1[a], Z);
      S = MFMA32(k2, asfrag(hh ? u32x4{0u, 0u, 0u, 0u} : qx), S);
      float mx = S[0];
#pragma unroll
      for (int i = 1; i < 16; ++i) mx = fmaxf(mx, S[i]);
      mx = fmaxf(mx, x32f(mx, lane));
      const unsigned nm = pk(-32.f - mx, 0.f);
      if (h & 1) qx[0] = nm; else qx[2] = nm;
      qs[a][0] = asfrag(hh ? u32x4{0u, 0u, 0u, 0u} : qx);
      qs[a][1] = asfrag(hh ? qx : u32x4{0u, 0u, 0u, 0u});
    }
    f32x16 SA = MFMA32(k1, q1[0], Z), SB;
    SA = MFMA32(k2, qs[0][0], SA);
    bf16x8 v0 = ldsr128(vl0), v1 = {};
    u32x4 P0, P1 = {};
#define EXPK(S, P, i, c) do { S[i] = __builtin_amdgcn_exp2f(S[i]); S[i + 1] = __builtin_amdgcn_exp2f(S[i + 1]); S[i + 2] = __builtin_amdgcn_exp2f(S[i + 2]); \
      S[i + 3] = __builtin_amdgcn_exp2f(S[i + 3]); P[c] = pk(S[i], S[i + 1]); P[c + 1] = pk(S[i + 2], S[i + 3]); PIN(P); SBAR(); } while (0)
#define TILE2(T, V0, V0N, K2, K2N, K1SRC, V1SRC, V0SRC, XLOAD, XOP) do { \
      SB = MFMA32(k1, q1[1], Z); V0N = ldsr128(V0SRC); XLOAD; SBAR(); EXPK(SA, P0, 0, 0); \
      Ob = MFMA32(v1, asfrag(P1), Ob); SBAR(); v1 = ldsr128(V1SRC); SBAR(); k1 = ldsr128(K1SRC); SBAR(); EXPK(SA, P0, 4, 2); \
      SB = MFMA32(K2, qs[1][T], SB); SBAR(); EXPK(SA, P1, 8, 0); \
      Oa = MFMA32(V0, asfrag(P0), Oa); SBAR(); EXPK(SA, P1, 12, 2); \
      SA = MFMA32(k1, q1[0], Z); SBAR(); EXPK(SB, P0, 0, 0); \
      Oa = MFMA32(v1, asfrag(P1), Oa); XOP; SBAR(); EXPK(SB, P0, 4, 2); \
      SA = MFMA32(K2N, qs[0][(T) ^ 1], SA); SBAR(); EXPK(SB, P1, 8, 0); \
      Ob = MFMA32(V0, asfrag(P0), Ob); SBAR(); EXPK(SB, P1, 12, 2); } while (0)
    bf16x8 v0b, k2b;
    SPIN_UNTIL(prog[P_KVL] >= 1);
#pragma unroll 1
    for (int G = 0; G < 16; ++G) {
      const int oc = (G & 3) * KVG, on = ((G + 1) & 3) * KVG;
      int pv;
      SBAR();
      TILE2(0, v0, v0b, k2, k2, kl + oc + 1024, vl1 + oc, vl0 + oc + KV_VT, (void)0, (void)0);
      TILE2(1, v0b, v0, k2, k2b, kl + oc + 2048, vl1 + oc + KV_VT, vl0 + oc + 2 * KV_VT, k2b = ldsr128(kl + oc + KV_K2 + 1024), (void)0);
      TILE2(0, v0, v0b, k2b, k2b, kl + oc + 3072, vl1 + oc + 2 * KV_VT, vl0 + oc + 3 * KV_VT, pv = prog[P_KVL], (void)0);
      if (__builtin_expect(rfl(pv) < G + 2, 0)) SPIN_UNTIL(prog[P_KVL] >= G + 2);
      asm volatile("" ::: "memory");
      SBAR();
      TILE2(1, v0b, v0, k2b, k2, kl + on, vl1 + oc + 3 * KV_VT, vl0 + on, k2 = ldsr128(kl + on + KV_K2), prog[P_KVC + w] = G + 1);
    }
    const float gq = *(const LAS float*)(lds + L_GATE + (w * 64 + lane) * 4);
    Ob = MFMA32(v1, asfrag(P1), Ob);
    {
      float la = hi32f(Oa[8]), lb = hi32f(Ob[8]), big = 0.f;
#pragma unroll
      for (int i = 0; i < 12; ++i) big += fabsf(Oa[i]) + fabsf(Ob[i]);
      if (__builtin_expect(__any(!(fminf(la, lb) >= 8.271806125530277e-25f && fmaxf(la, lb) <= 1.8446744073709552e19f && big <= 3.0e38f)), 0)) {
#pragma unroll 1
        for (int a = 0; a < 2; ++a) {
          f32x16 O;
          attn_slow(kvh, a ? q1[1] : q1[0], a ? q2[1] : q2[0], h & 1, lane, O);
          if (a) Ob = O; else Oa = O;
        }
        la = hi32f(Oa[8]); lb = hi32f(Ob[8]);
      }
      const float ia = (1.f - lo32f(gq)) * __builtin_amdgcn_rcpf(la), ib = (1.f - hi32f(gq)) * __builtin_amdgcn_rcpf(lb);
      const unsigned ob = qt0 * 40 * 1024;
      ST128((u32x4{pk(Oa[0] * ia, Oa[1] * ia), pk(Oa[2] * ia, Oa[3] * ia), pk(Oa[4] * ia, Oa[5] * ia), pk(Oa[6] * ia, Oa[7] * ia)}), rsO, lane * 16, ob + h * 1024);
      ST128((u32x4{pk(Ob[0] * ib, Ob[1] * ib), pk(Ob[2] * ib, Ob[3] * ib), pk(Ob[4] * ib, Ob[5] * ib), pk(Ob[6] * ib, Ob[7] * ib)}), rsO, lane * 16, ob + 40960 + h * 1024);
      if (hh == 0) {
        ST64((u32x2{pk(Oa[8] * ia, Oa[9] * ia), pk(Oa[10] * ia, Oa[11] * ia)}), rsO, r * 8, ob + 32768 + h * 256);
        ST64((u32x2{pk(Ob[8] * ib, Ob[9] * ib), pk(Ob[10] * ib, Ob[11] * ib)}), rsO, r * 8, ob + 40960 + 32768 + h * 256);
      }
    }
    publish(prog, 3, ws, C_O + j * 4 + (h >> 3), lane);
  } else if (w < 8) {
    const int m = w - 4, c = lane & 15, g = lane >> 4;
    const prog_t pl = prog + P_LAND + (lane & 3);
    {
      const int sw = c * 128 + ((g ^ ((c >> 1) & 7)) << 4);
      f32x4 acc[2][5] = {};
      bf16x8 xf[2][2], hf[2][5];
#define HRD(sb, u, n) do { if ((n) < 2) xf[u][n] = ldsr128((sb) + R_X + m * 4096 + (sw ^ ((u) * 64)) + (n) * 2048); \
        else hf[u][(n) - 2] = ldsr128((sb) + R_H + (sw ^ ((u) * 64)) + ((n) - 2) * 2048); } while (0)
#define HMF(u, n) acc[(n) & 1][(n) >> 1] = MFMA16(xf[u][(n) & 1], hf[u][(n) >> 1], acc[(n) & 1][(n) >> 1])
      SPIN_UNTIL(__all(*pl > 10));
#pragma unroll
      for (int n = 0; n < 7; ++n) HRD(lds, 0, n);
      int slot = 0;
#pragma unroll 1
      for (int t = 10; t < 20; ++t) {
        const LAS char* sb = lds + slot * PS;
        slot = slot == 2 ? 0 : slot + 1;
        const LAS char* sn = lds + slot * PS;
        int pv;
        SBAR();
#pragma unroll
        for (int n = 0; n < 10; ++n) { if (n < 7) HRD(sb, 1, n); if (n == 7) pv = *pl; HMF(0, n); SBAR(); }
        asm volatile("s_waitcnt lgkmcnt(0)" ::: "memory");
        prog[P_CONS + w] = t + 1;
        if (t < 19 && __builtin_expect(!__all(pv > t + 1), 0)) SPIN_UNTIL(__all(*pl > t + 1));
        asm volatile("" ::: "memory");
        SBAR();
#pragma unroll
        for (int n = 0; n < 10; ++n) { if (n < 7) HRD(sn, 0, n); HMF(1, n); SBAR(); }
      }
      SPIN_UNTIL(prog[P_KVL] >= 1);
      __builtin_amdgcn_s_setprio(3);
      const rsrc_t rsAB = mkrs(ws + WS_AB, 40 * 128 * 1024);
#pragma unroll
      for (int i = 0; i < 5; ++i) {
        const int e16 = cb * 5 + i;
        const unsigned so = ((e16 >> 1) * 128 + (rb * 4 + m) * 2) * 1024 + (e16 & 1) * 256;
        ST128(merge16(pk4(acc[0][i]), pk4(acc[1][i])), rsAB, (g & 1) * 1024 + (g >> 1) * 512 + c * 16, so);
      }
      const unsigned tag = get_tag(prog);
      if (drain_last(prog, 1, 4, lane) && lane == 0)
        __hip_atomic_store((GAS unsigned*)(ws + WS_FLAG1 + ((cb >> 1) * 2 + (rb >> 3)) * 256 + ((rb & 7) * 2 + (cb & 1)) * 4), tag, __ATOMIC_RELAXED, __HIP_MEMORY_SCOPE_AGENT);
    }
    const int wm = m & 1, wn = m >> 1;
    const int ro = R_X + (wm * 64 + c) * 128 + ((g ^ ((c >> 1) & 7)) << 4);
    const int ao = R_B + g * 512 + c * 16 + wn * (2 * 4096 + 256);
    const int ao1 = ao + (wn ? 4096 - 256 : 256);
    f32x4 acc[5][4] = {};
    bf16x8 fa[2][5], fc[2][4];
    int slot = 2;
    __builtin_amdgcn_s_setprio(G_BOOST ? 3 : PRIO_G);
    SPIN_LAZY(__all(*pl > 20));
#define RD1(sb, b, n) do { if ((n) < 4) fc[b][n] = ldsr128((sb) + (ro ^ ((b) * 64)) + (n) * 2048); \
      else fa[b][(n) - 4] = ldsr128((sb) + ((((n) - 4) & 1) ? ao1 : ao) + (((n) - 4) >> 1) * 4096 + (b) * 2048); } while (0)
#define MF(b, n) acc[(n) >> 2][(n) & 3] = MFMA16(fa[b][(n) >> 2], fc[b][(n) & 3], acc[(n) >> 2][(n) & 3])
#pragma unroll
    for (int n = 0; n < 9; ++n) RD1(lds + slot * RS, 0, n);
#pragma unroll 1
    for (int t = 20; t < 36; ++t) {
      const LAS char* sb = lds + slot * RS;
      slot = slot == 2 ? 0 : slot + 1;
      const LAS char* sn = lds + slot * RS;
      int pv;
      if (G_BOOST && t == 20 + G_BOOST) __builtin_amdgcn_s_setprio(PRIO_G);
      SBAR();
#pragma unroll
      for (int n = 0; n < 20; ++n) {
        MF(0, n);
        if (n < 9) RD1(sb, 1, n);
        if (n == 13) { asm volatile("s_waitcnt lgkmcnt(0)" ::: "memory"); prog[P_CONS + w] = t + 1; }
        if (n == 17) pv = *pl;
        SBAR();
      }
      if (__builtin_expect(!__all(pv > t + 1), 0)) SPIN_UNTIL(__all(*pl > t + 1));
      asm volatile("" ::: "memory");
      SBAR();
#pragma unroll
      for (int n = 0; n < 20; ++n) {
        MF(1, n);
        if (n < 9) RD1(sn, 0, n);
        SBAR();
      }
    }
    __builtin_amdgcn_s_setprio(3);
    const rsrc_t rsY = mkrs(ws + WS_Y, 4 * YPART);
    const unsigned yb = (part * 2 + kh) * YPART + ((mt * 2 + wm) * 8 + nt * 2 + wn) * 10240;
#pragma unroll
    for (int q = 0; q < 4; ++q)
#pragma unroll
      for (int i = 0; i < 4; i += 2) {
        const u32x2 a = pk4(acc[i][q]), b = pk4(acc[i + 1][q]);
        ST128((u32x4{a[0], a[1], b[0], b[1]}), rsY, lane * 16, yb + (q * 2 + (i >> 1)) * 1024);
      }
#pragma unroll
    for (int q = 0; q < 4; q += 2) {
      const u32x2 a = pk4(acc[4][q]), b = pk4(acc[4][q + 1]);
      ST128((u32x4{a[0], a[1], b[0], b[1]}), rsY, lane * 16, yb + (8 + (q >> 1)) * 1024);
    }
    publish(prog, 2, ws, C_Y + mt * 4 + nt, lane);
    __builtin_amdgcn_s_setprio(0);
  } else {
    const int m = w - 8;
    const unsigned vl = lane * 16;
    const unsigned swz = ((lane & 7) ^ (lane >> 4)) << 4;
    const prog_t pc = prog + P_CONS + (lane & 7);
    if (m == 0) {
      SPIN_UNTIL(__all(*pc >= 20));
      {
        const unsigned cv = (lane & 4) ? 0u : 0x3F803F80u;
        LAS char* cp = (LAS char*)smem + L_KV + (lane >> 5) * KVG + KV_V + ((lane >> 3) & 3) * KV_VT + 1280 + (lane & 7) * 16;
        *(LAS u32x4*)cp = u32x4{cv, cv, cv, cv};
        *(LAS u32x4*)(cp + 2 * KVG) = u32x4{cv, cv, cv, cv};
      }
      SPIN_UNTIL(prog[P_CNT] >= 4);
      const unsigned tag = get_tag(prog);
      const rsrc_t rsF = mkrs(ws + WS_FLAG + h * 512, 256);
      const rsrc_t rsKV = mkrs(kvh, 16 * KVG);
      const prog_t pa = prog + P_KVC + (lane & 3);
      unsigned ready = 0;
#pragma unroll 1
      for (int G = -2; G < 16; ++G) {
        if (G >= 0) {
          if (G < 15) asm volatile("s_waitcnt vmcnt(12) lgkmcnt(0)" ::: "memory"); else asm volatile("s_waitcnt vmcnt(0)" ::: "memory");
          prog[P_KVL] = G < 15 ? G + 1 : 17;
        }
        if (G + 2 < 16) {
          const int gi = G + 2;
#pragma unroll 1
          for (int it = 0; it < (1 << 18) && !((ready >> gi) & 1); ++it) {
            const u32x4 f = __builtin_amdgcn_raw_buffer_load_b128(rsF, vl, 0, 16);
            ready = (unsigned)__ballot(f[0] == tag && f[1] == tag && f[2] == tag && f[3] == tag) & 0xFFFFu;
            asm volatile("s_waitcnt vmcnt(0)" ::: "memory");
            if (G >= -1) prog[P_KVL] = G + 2;
            if (!((ready >> gi) & 1)) __builtin_amdgcn_s_sleep(POLL_SLP);
          }
          SPIN_LAZY(__all(*pa >= G - 1));
          if (G == 13) {
            SPIN_UNTIL(prog[P_GO] != 0);
            dma16c<0>(ldsb + L_GATE, vl, mkrs(ws + WS_G + j * 1024, 1024), 0);
          }
          const unsigned d_ = ldsb + L_KV + ((G + 2) & 3) * KVG, s_ = gi * KVG;
          dma16c<0>(d_, vl, rsKV, s_); dma16c<0>(d_ + 4096, vl, rsKV, s_ + 4096);
          dma16m<2048, 6>(d_ + 4096, vl, rsKV, s_ + 4096); dma16m<3072, 7>(d_ + 4096, vl, rsKV, s_ + 4096);
          dma16c<1024>(d_, vl, rsKV, s_); dma16c<2048>(d_, vl, rsKV, s_); dma16c<3072>(d_, vl, rsKV, s_); dma16c<1024>(d_ + 4096, vl, rsKV, s_ + 4096);
          dma16m<0, 8>(d_ + 8192, vl, rsKV, s_ + 8192); dma16m<1024, 9>(d_ + 8192, vl, rsKV, s_ + 8192);
          dma16m<2048, 10>(d_ + 8192, vl, rsKV, s_ + 8192); dma16m<3072, 11>(d_ + 8192, vl, rsKV, s_ + 8192);
          if (G == -2) {
            asm volatile("s_waitcnt vmcnt(8) lgkmcnt(0)" ::: "memory");
            prog[P_KV0] = 1;
            asm volatile("s_waitcnt vmcnt(0)" ::: "memory");
            prog[P_KVL] = 1;
          }
        }
      }
    } else if (m == 1) {
      const rsrc_t rsC = mkrs((part ? ci : cr) + (size_t)(mt * 128) * SEQ + kh * 1024, 128 * SEQ * 2 - kh * 2048);
      const unsigned vc0 = (lane >> 3) * 4096 + swz, vc1 = (vc0 ^ 64) + 8 * 4096;
      SPIN_UNTIL(prog[P_CNT + 1] >= 4);
      await_flags(prog, ws + WS_FLAG2 + xcd * 256, lane < 4, lane);
      prog[P_GO] = 1;
      touch(ldsb + L_SCR, lane * 128, mkrs((const char*)wo + bid * 3200, 3200));
      int slot = 2;
#pragma unroll 1
      for (int t = 18; t < 36; ++t) {
        if (t >= 20) {
          if (t < 35) asm volatile("s_waitcnt vmcnt(16)" ::: "memory"); else asm volatile("s_waitcnt vmcnt(0)" ::: "memory");
          prog[P_LAND + 1] = t < 35 ? t + 1 : 37;
        }
        const int s = t + 2;
        if (s < 36) {
          SPIN_LAZY(__all(*pc >= (t < 20 ? 20 : t)));
          const unsigned sb = ldsb + slot * RS + R_X, so = (s - 20) * 128;
#pragma unroll
          for (int q = 0; q < 8; ++q) { dma16<0>(sb + q * 2048, vc0, rsC, so + q * 16 * 4096); dma16<0>(sb + q * 2048 + 1024, vc1, rsC, so + q * 16 * 4096); }
          slot = slot == 2 ? 0 : slot + 1;
        }
      }
    } else {
      const int hf = m - 2;
      SPIN_UNTIL(prog[P_CNT + 1] >= 4);
      const unsigned tag = get_tag(prog);
      const rsrc_t rsF = mkrs(ws + WS_FLAG1 + ((part * 4 + nt) * 2 + kh) * 256, 64);
      unsigned ready = 0;
      const rsrc_t rsA = mkrs(ws + WS_AB + ((size_t)(part * 20 + nt * 5) * 128 + kh * 64 + hf * 2) * 1024, (5 * 128 - kh * 64 - hf * 2) * 1024);
      int slot = 2;
#pragma unroll 1
      for (int t = 18; t < 36; ++t) {
        if (t >= 20) {
          if (t < 35) asm volatile("s_waitcnt vmcnt(10)" ::: "memory"); else asm volatile("s_waitcnt vmcnt(0)" ::: "memory");
          prog[P_LAND + m] = t < 35 ? t + 1 : 37;
        }
        const int s = t + 2;
        if (s < 36) {
#pragma unroll 1
          for (int it = 0; it < (1 << 18) && !((ready >> ((s - 20) >> 1)) & 1); ++it) {
            const u32x2 f = __builtin_amdgcn_raw_buffer_load_b64(rsF, lane * 8, 0, 16);
            ready = (unsigned)__ballot(f[0] == tag && f[1] == tag) & 0xFFu;
            asm volatile("s_waitcnt vmcnt(0)" ::: "memory");
            if (t >= 19) prog[P_LAND + m] = t + 2;
            if (!((ready >> ((s - 20) >> 1)) & 1)) __builtin_amdgcn_s_sleep(POLL_SLP);
          }
          SPIN_LAZY(__all(*pc >= (t < 20 ? 20 : t)));
          const unsigned sb = ldsb + slot * RS + R_B + hf * 2048;
#pragma unroll
          for (int i = 0; i < 5; ++i) { dma16c<0>(sb + i * 4096, vl, rsA, (i * 128 + (s - 20) * 4) * 1024); dma16c<1024>(sb + i * 4096, vl, rsA, (i * 128 + (s - 20) * 4) * 1024); }
          slot = slot == 2 ? 0 : slot + 1;
        }
      }
    }
    SPIN_LAZY(__all(*pc >= 34));
    __builtin_amdgcn_s_setprio(3);
    {
      const rsrc_t rsW = mkrs((const char*)wo + fnt * 160, DM * DM * 2 - fnt * 160);
      unsigned vm[5], vf[5];
#pragma unroll
      for (int b = 0; b < 5; ++b) {
        const int gi = b * 64 + lane, pr = gi / 10, pp = gi % 10, rho = pr ^ (((pr >> 3) & 1) << 2), kq = rho >> 3, jj = (rho >> 2) & 1, j3 = rho & 3;
        vm[b] = (20 * (kq >> 1) + 8 * jj + 4 * (kq & 1) + j3) * 1280 + pp * 16;
        vf[b] = (40 * kq + 20 * jj + 16 + j3) * 1280 + pp * 16;
      }
#pragma unroll 1
      for (int v = m - 1; v < 20 && m > 0; v += 3) {
        SPIN_LAZY(__all(*pc >= (v < 7 ? 34 : (v < 14 ? 35 : 36))));
#pragma unroll
        for (int b = 0; b < 5; ++b) dma16<0>(ldsb + (v * 5 + b) * 1024, v < 16 ? vm[b] : vf[b], rsW, v < 16 ? v * 40 * 1280 : (v - 16) * 160 * 1280);
      }
    }
    asm volatile("s_waitcnt vmcnt(0)" ::: "memory");
    if (m > 0) prog[P_WO - 1 + m] = 1;
  }
  if (w < 8) final_gemm(smem, prog, ws, w, lane, fmt);
  else final_seed(smem, prog, ws, w - 8, lane, fmt, fnt);
  WAIT_BAR(0);
  {
    if (w < 10) {
      const LAS char* red = lds + lane * 16;
#pragma unroll
      for (int a = 0; a < 2; ++a) {
        const int tile = w * 2 + a, rt = tile / 5, i = tile - rt * 5;
        f32x4 s = *(const LAS f32x4*)(red + red_off(0, tile));
#pragma unroll
        for (int q = 1; q < 5; ++q) s += *(const LAS f32x4*)(red + red_off(q, tile));
        *(u32x2*)(out + (size_t)(fmt * 64 + rt * 16 + (lane & 15)) * DM + fnt * 80 + i * 16 + 4 * (lane >> 4)) = pk4(s);
      }
    }
    if (bid == 0 && w == 11) {
      await(prog, 3, ws, C_O + (lane & 31), 8);
      if (lane == 0) __hip_atomic_store((GAS unsigned*)(ws + WS_EPOCH), (unsigned)prog[P_EP] + 1u, __ATOMIC_RELAXED, __HIP_MEMORY_SCOPE_AGENT);
    }
  }
}

extern "C" void kernel_launch(void* const* d_in, const int* in_sizes, int n_in, void* d_out, int out_size, void* d_ws, size_t ws_size,
                              hipStream_t stream) {
  const u16* x = (const u16*)d_in[0];
  const u16* cr = (const u16*)d_in[1];
  const u16* ci = (const u16*)d_in[2];
  const u16* hr = (const u16*)d_in[3];
  const u16* hi = (const u16*)d_in[4];
  const u16* wqkv = (const u16*)d_in[6];
  const u16* wo = (const u16*)d_in[7];
  const u16* gw = (const u16*)d_in[8];
  char* ws = (char*)d_ws;
  fused_kernel<<<256, 768, 0, stream>>>(x, wqkv, hr, hi, ws, gw, cr, ci, wo, (u16*)d_out);
}
```
